```python
import torch

def _softmax(x, axis=-1):
    return torch.softmax(x, dim=axis)
def _gelu(x):
    return torch.nn.functional.gelu(x, approximate="tanh")
def _silu(x):
    return torch.nn.functional.silu(x)
def _sigmoid(x):
    return torch.sigmoid(x)
def _layernorm(x, axis=-1, eps=1e-5):
    m = x.mean(dim=axis, keepdim=True)
    v = x.var(dim=axis, keepdim=True, unbiased=False)
    return (x - m) / torch.sqrt(v + eps)
def _scatter_add(tgt, idx, src, axis=0):
    return tgt.clone().index_add_(axis, idx, src)
def _segment_sum(src, seg, n):
    out = torch.zeros((n,) + src.shape[1:], dtype=src.dtype)
    return out.index_add_(0, seg, src)
def _pad(x, pw):
    flat = tuple(v for pair in reversed(pw) for v in pair)
    return torch.nn.functional.pad(x, flat)

kernel_name = 'standalone__self_sup_conv_stem_hubert__speech'

def setup_inputs():

    _w = (lambda a: (lambda t: t.to(torch.bfloat16) if t.is_floating_point() else t)(torch.as_tensor(a)))
    B, T, D, K = 2, 4000, 512, 3
    scale = (1.0 / (K * D)) ** 0.5
    return {
        "x": _w((lambda *s: torch.randn(*s, dtype=torch.bfloat16) * 0.05)(B, T, D)),
        "w": _w((lambda *s: torch.randn(*s, dtype=torch.bfloat16) * 0.05)(K, D, D) * scale),
        "b": _w((lambda *s: torch.randn(*s, dtype=torch.bfloat16) * 0.05)(D) * 0.1),
        "gamma": _w(torch.ones((D,), dtype=float)),
        "beta": _w(torch.zeros((D,), dtype=float)),
    }

def reference(x, w, b, gamma, beta):
    B, T, D = x.shape
    K = w.shape[0]
    s = 2
    L = (T - K) // s + 1
    y = torch.einsum('blc,cd->bld', x[:, 0 : s * L : s, :], w[0])
    for i in range(1, K):
        y = y + torch.einsum('blc,cd->bld', x[:, i : i + s * L : s, :], w[i])
    y = y + b[None, None, :]
    mu = y.mean(1, keepdims=True)
    var = ((y - mu) ** 2).mean(1, keepdims=True)
    yn = (y - mu) / torch.sqrt(var + 1e-5)
    yn = yn * gamma[None, None, :] + beta[None, None, :]
    return _gelu(yn)
```

```cpp
#include <hip/hip_runtime.h>
#include <hip/hip_bf16.h>


typedef __attribute__((ext_vector_type(4))) __bf16 bf16x4;
typedef __attribute__((ext_vector_type(8))) __bf16 bf16x8;
typedef __attribute__((ext_vector_type(16))) float f32x16;
typedef __attribute__((ext_vector_type(4))) unsigned u32x4;
#define GAS __attribute__((address_space(1)))
#define LAS __attribute__((address_space(3)))
typedef GAS unsigned gu32;
typedef GAS unsigned long long gu64;
#define RLX_AGENT __ATOMIC_RELAXED, __HIP_MEMORY_SCOPE_AGENT

constexpr int T_ = 4000, D_ = 512, L_ = 1999;
constexpr int XE_OFF = 0, XO_OFF = 17408, W_OFF = 33792, STAGE_B = 58368;
constexpr int NSTAGE = 8;
constexpr int GRAN_OFF = 8192;

__device__ __forceinline__ bf16x4 tr_read(unsigned addr) {
  bf16x4 r;
  asm volatile("ds_read_b64_tr_b16 %0, %1" : "=v"(r) : "v"(addr));
  return r;
}

__device__ __forceinline__ void merge(float& n, float& mean, float& m2, float nb, float meanb, float m2b) {
  float nt = n + nb;
  float inv = nt > 0.f ? 1.0f / nt : 0.f;
  float d = meanb - mean;
  mean = mean + d * nb * inv;
  m2 = m2 + m2b + d * d * n * nb * inv;
  n = nt;
}

__global__ void __launch_bounds__(256, 1)
conv_stem_kernel(const __hip_bfloat16* __restrict__ x, const __hip_bfloat16* __restrict__ w,
                 const __hip_bfloat16* __restrict__ bias, const __hip_bfloat16* __restrict__ gamma,
                 const __hip_bfloat16* __restrict__ beta, __hip_bfloat16* __restrict__ out,
                 unsigned* __restrict__ ws) {
  __shared__ __attribute__((aligned(16))) char smem[2 * STAGE_B];
  const int tid = threadIdx.x;
  const int lane = tid & 63;
  const int wave = __builtin_amdgcn_readfirstlane(tid >> 6);
  const int wg = blockIdx.x;
  const int xcd = wg & 7, idx = wg >> 3;
  const int b = xcd >> 2, mt = (xcd & 3) * 4 + (idx >> 3), nt = idx & 7;
  const int grp = b * 8 + nt;
  const int l0 = mt * 128;
  const int nvalid = min(128, L_ - l0);

  gu32* epoch_p = (gu32*)ws + grp * 64;
  const unsigned epoch = __hip_atomic_load(epoch_p, RLX_AGENT) + 1u;

  const int wm = wave >> 1, wn = wave & 1;
  const int col = wn * 32 + (lane & 31);
  const int h = lane >> 5;
  const float bia = __bfloat162float(bias[nt * 64 + col]);
  const float gam = __bfloat162float(gamma[nt * 64 + col]);
  const float bet = __bfloat162float(beta[nt * 64 + col]);

  const int prow = lane >> 3, pch = lane & 7;
  const char* xb = (const char*)x;
  const char* wb = (const char*)w;
  unsigned xoff[9];
#pragma unroll
  for (int i = 0; i < 9; ++i) {
    int j, par;
    if (i < 4) { j = (wave * 4 + i) * 8 + prow; par = 0; }
    else if (i < 8) { j = (wave * 4 + i - 4) * 8 + prow; par = 1; }
    else { j = 128 + prow; par = 0; }
    int row = min(b * T_ + 2 * l0 + 2 * j + par, 2 * T_ - 1);
    xoff[i] = (unsigned)row * 1024u + (unsigned)((pch ^ ((j >> 1) & 7)) * 16);
  }
  unsigned woff[6];
#pragma unroll
  for (int i = 0; i < 6; ++i) {
    int r = (wave * 6 + i) * 8 + prow;
    int tap = r >> 6, k = r & 63;
    woff[i] = (unsigned)(tap * 512 + k) * 1024u + (unsigned)(nt * 128) + (unsigned)((pch ^ (((k >> 1) & 1) << 2)) * 16);
  }

  auto stage_load = [&](int s) {
    char* base = smem + (s & 1) * STAGE_B;
    const char* xs = xb + s * 128;
    const char* wsrc = wb + (size_t)s * 65536;
    if (wave == 0)
      __builtin_amdgcn_global_load_lds((const void*)(xs + xoff[8]), (void*)(base + XE_OFF + 16 * 1024), 16, 0, 0);
#pragma unroll
    for (int i = 0; i < 4; ++i)
      __builtin_amdgcn_global_load_lds((const void*)(xs + xoff[i]), (void*)(base + XE_OFF + (wave * 4 + i) * 1024), 16, 0, 0);
#pragma unroll
    for (int i = 0; i < 6; ++i)
      __builtin_amdgcn_global_load_lds((const void*)(wsrc + woff[i]), (void*)(base + W_OFF + (wave * 6 + i) * 1024), 16, 0, 0);
#pragma unroll
    for (int i = 0; i < 4; ++i)
      __builtin_amdgcn_global_load_lds((const void*)(xs + xoff[4 + i]), (void*)(base + XO_OFF + (wave * 4 + i) * 1024), 16, 0, 0);
  };

  const unsigned sbase = (unsigned)(size_t)(LAS char*)smem;
  const int blk = (lane >> 4) & 1, q = (lane & 15) >> 2, p = lane & 3;

  f32x16 acc0 = {}, acc1 = {};

  stage_load(0);
  stage_load(1);

#pragma unroll 1
  for (int s = 0; s < NSTAGE; ++s) {
    if (s < NSTAGE - 1) asm volatile("s_waitcnt vmcnt(14)" ::: "memory");
    else asm volatile("s_waitcnt vmcnt(0)" ::: "memory");
    __builtin_amdgcn_s_barrier();
    const char* base = smem + (s & 1) * STAGE_B;
    const unsigned ubase = sbase + (s & 1) * STAGE_B;
#pragma unroll
    for (int tap = 0; tap < 3; ++tap) {
      const int plane = (tap & 1) ? XO_OFF : XE_OFF;
      const int j0 = wm * 64 + (lane & 31) + (tap >> 1);
      const int j1 = j0 + 32;
#pragma unroll
      for (int ks = 0; ks < 4; ++ks) {
        const int ch = 2 * ks + h;
        bf16x8 a0 = *(const bf16x8*)(base + plane + j0 * 128 + ((ch ^ ((j0 >> 1) & 7)) * 16));
        bf16x8 a1 = *(const bf16x8*)(base + plane + j1 * 128 + ((ch ^ ((j1 >> 1) & 7)) * 16));
        const int r0 = 16 * ks + 8 * h + q;
        const unsigned bc = (unsigned)((wn * 64 + 32 * blk + 8 * p) ^ (((r0 >> 1) & 1) << 6));
        const unsigned ba = ubase + W_OFF + tap * 8192 + r0 * 128 + bc;
        bf16x4 t0 = tr_read(ba);
        bf16x4 t1 = tr_read(ba + 512);
        asm volatile("s_waitcnt lgkmcnt(0)" ::: "memory");
        __builtin_amdgcn_sched_barrier(0);
        bf16x8 bb = __builtin_shufflevector(t0, t1, 0, 1, 2, 3, 4, 5, 6, 7);
        acc0 = __builtin_amdgcn_mfma_f32_32x32x16_bf16(a0, bb, acc0, 0, 0, 0);
        acc1 = __builtin_amdgcn_mfma_f32_32x32x16_bf16(a1, bb, acc1, 0, 0, 0);
      }
    }
    asm volatile("s_waitcnt lgkmcnt(0)" ::: "memory");
    __builtin_amdgcn_s_barrier();
    if (s + 2 < NSTAGE) stage_load(s + 2);
  }

  float v[32];
#pragma unroll
  for (int r = 0; r < 16; ++r) { v[r] = acc0[r] + bia; v[16 + r] = acc1[r] + bia; }
  float cnt = 0.f, sum = 0.f;
#pragma unroll
  for (int r = 0; r < 32; ++r) {
    int row = wm * 64 + (r >> 4) * 32 + (r & 3) + 8 * ((r & 15) >> 2) + 4 * h;
    bool ok = row < nvalid;
    cnt += ok ? 1.f : 0.f;
    sum += ok ? v[r] : 0.f;
  }
  float mean = cnt > 0.f ? sum / cnt : 0.f;
  float m2 = 0.f;
#pragma unroll
  for (int r = 0; r < 32; ++r) {
    int row = wm * 64 + (r >> 4) * 32 + (r & 3) + 8 * ((r & 15) >> 2) + 4 * h;
    float d = v[r] - mean;
    m2 += row < nvalid ? d * d : 0.f;
  }
  {
    float nb = __shfl_xor(cnt, 32), mb = __shfl_xor(mean, 32), qb = __shfl_xor(m2, 32);
    merge(cnt, mean, m2, nb, mb, qb);
  }
  float* sA = (float*)smem;
  float2* sB = (float2*)(smem + 4096);
  char* sO = smem + 16384;
  if (h == 0) { float* d = sA + (wm * 64 + col) * 4; d[0] = cnt; d[1] = mean; d[2] = m2; }
  __syncthreads();
  gu64* gran = (gu64*)((char*)ws + GRAN_OFF) + (size_t)grp * 2048;
  if (tid < 128) {
    int c = tid >> 1;
    float n0 = sA[c * 4], me = sA[c * 4 + 1], q0 = sA[c * 4 + 2];
    merge(n0, me, q0, sA[(64 + c) * 4], sA[(64 + c) * 4 + 1], sA[(64 + c) * 4 + 2]);
    float val = (tid & 1) ? q0 : me;
    __hip_atomic_store(gran + (c * 16 + mt) * 2 + (tid & 1),
                       ((unsigned long long)epoch << 32) | __float_as_uint(val), RLX_AGENT);
  }
  {
    gu64* g = gran + tid * 8;
    unsigned vv[8];
    for (unsigned spins = 0;; ++spins) {
      bool ok = true;
#pragma unroll
      for (int k = 0; k < 8; ++k) {
        unsigned long long xx = __hip_atomic_load(g + k, RLX_AGENT);
        vv[k] = (unsigned)xx;
        ok &= (unsigned)(xx >> 32) == epoch;
      }
      if (__all(ok) || spins > (1u << 22)) break;
      __builtin_amdgcn_s_sleep(1);
    }
#pragma unroll
    for (int k = 0; k < 4; ++k)
      sB[((tid & 3) * 4 + k) * 64 + (tid >> 2)] = make_float2(__uint_as_float(vv[2 * k]), __uint_as_float(vv[2 * k + 1]));
  }
  __syncthreads();
  if (tid == 0) __hip_atomic_store(epoch_p, epoch, RLX_AGENT);
  float gmean, rstd;
  {
    float2 st[16];
#pragma unroll
    for (int i = 0; i < 16; ++i) st[i] = sB[i * 64 + col];
    float sm = 0.f;
#pragma unroll
    for (int i = 0; i < 16; ++i) sm += st[i].x * (i == 15 ? (float)(L_ - 15 * 128) : 128.f);
    gmean = sm * (1.0f / L_);
    float M2 = 0.f;
#pragma unroll
    for (int i = 0; i < 16; ++i) {
      float d = st[i].x - gmean;
      M2 += st[i].y + d * d * (i == 15 ? (float)(L_ - 15 * 128) : 128.f);
    }
    rstd = rsqrtf(M2 * (1.0f / L_) + 1e-5f);
  }
  const float sc = rstd * gam;
#pragma unroll
  for (int r = 0; r < 32; ++r) {
    int row = wm * 64 + (r >> 4) * 32 + (r & 3) + 8 * ((r & 15) >> 2) + 4 * h;
    float yn = (v[r] - gmean) * sc + bet;
    float u = 0.7978845608028654f * (yn + 0.044715f * yn * yn * yn);
    float e = __expf(2.f * u);
    float th = 1.f - 2.f / (e + 1.f);
    float o = 0.5f * yn * (1.f + th);
    *(__hip_bfloat16*)(sO + row * 128 + col * 2) = __float2bfloat16(o);
  }
  __syncthreads();
#pragma unroll
  for (int i = 0; i < 4; ++i) {
    int row = i * 32 + (tid >> 3);
    if (row < nvalid) {
      u32x4 d = *(const u32x4*)(sO + row * 128 + (tid & 7) * 16);
      *(u32x4*)((char*)out + ((size_t)(b * L_ + l0 + row) * 512 + nt * 64) * 2 + (tid & 7) * 16) = d;
    }
  }
}

extern "C" void kernel_launch(void* const* d_in, const int* in_sizes, int n_in,
                              void* d_out, int out_size, void* d_ws, size_t ws_size,
                              hipStream_t stream) {
  conv_stem_kernel<<<256, 256, 0, stream>>>(
      (const __hip_bfloat16*)d_in[0], (const __hip_bfloat16*)d_in[1], (const __hip_bfloat16*)d_in[2],
      (const __hip_bfloat16*)d_in[3], (const __hip_bfloat16*)d_in[4], (__hip_bfloat16*)d_out, (unsigned*)d_ws);
}
```

```cpp
#include <hip/hip_runtime.h>
#include <hip/hip_bf16.h>

typedef __attribute__((ext_vector_type(4))) __bf16 bf16x4;
typedef __attribute__((ext_vector_type(8))) __bf16 bf16x8;
typedef __attribute__((ext_vector_type(16))) float f32x16;
typedef __attribute__((ext_vector_type(4))) float f32x4;
typedef __attribute__((ext_vector_type(4))) unsigned u32x4;
#define GAS __attribute__((address_space(1)))
#define LAS __attribute__((address_space(3)))
typedef GAS unsigned gu32;
typedef GAS unsigned long long gu64;
#define RLX_AGENT __ATOMIC_RELAXED, __HIP_MEMORY_SCOPE_AGENT

constexpr int T_ = 4000, D_ = 512, L_ = 1999;
constexpr int XE_OFF = 0, XO_OFF = 17408, W_OFF = 33792, STAGE_B = 58368;
constexpr int NSTAGE = 8;
constexpr int GRAN_OFF = 8192;

#define STAMPT(i)

__device__ __forceinline__ bf16x4 tr_read(unsigned addr, int off) {
  bf16x4 r;
  asm volatile("ds_read_b64_tr_b16 %0, %1 offset:%2" : "=v"(r) : "v"(addr), "i"(off));
  return r;
}
__device__ __forceinline__ bf16x8 rd128(unsigned addr, int off) {
  bf16x8 r;
  asm volatile("ds_read_b128 %0, %1 offset:%2" : "=v"(r) : "v"(addr), "i"(off));
  return r;
}

__device__ __forceinline__ void merge(float& n, float& mean, float& m2, float nb, float meanb, float m2b) {
  float nt = n + nb;
  float inv = nt > 0.f ? __builtin_amdgcn_rcpf(nt) : 0.f;
  float d = meanb - mean;
  mean = mean + d * nb * inv;
  m2 = m2 + m2b + d * d * n * nb * inv;
  n = nt;
}

__global__ void __launch_bounds__(512, 2)
conv_stem_kernel(const __hip_bfloat16* __restrict__ x, const __hip_bfloat16* __restrict__ w,
                 const __hip_bfloat16* __restrict__ bias, const __hip_bfloat16* __restrict__ gamma,
                 const __hip_bfloat16* __restrict__ beta, __hip_bfloat16* __restrict__ out,
                 unsigned* __restrict__ ws) {
  __shared__ __attribute__((aligned(16))) char smem[2 * STAGE_B];
  const int tid = threadIdx.x;
  const int lane = tid & 63;
  const int wave = __builtin_amdgcn_readfirstlane(tid >> 6);
  const int wg = blockIdx.x;
  const int xcd = wg & 7, idx = wg >> 3;
  const int b = xcd >> 2, mt = (xcd & 3) * 4 + (idx >> 3), nt = idx & 7;
  const int grp = b * 8 + nt;
  const int l0 = mt * 128;
  const int nvalid = min(128, L_ - l0);
  STAMPT(0);

  gu32* epoch_p = (gu32*)ws + grp * 64;
  const unsigned epoch = __hip_atomic_load(epoch_p, RLX_AGENT) + 1u;

  const int kh = wave >> 2, wm = (wave >> 1) & 1, wn = wave & 1;
  const int col = wn * 32 + (lane & 31);
  const int h = lane >> 5;
  const float bia = __bfloat162float(bias[nt * 64 + col]);
  const float gam = __bfloat162float(gamma[nt * 64 + col]);
  const float bet = __bfloat162float(beta[nt * 64 + col]);

  const int prow = lane >> 3, pch = lane & 7;
  const char* xb = (const char*)x;
  const char* wb = (const char*)w;
  unsigned xoff[5];
#pragma unroll
  for (int i = 0; i < 5; ++i) {
    int j, par;
    if (i < 2) { j = (wave * 2 + i) * 8 + prow; par = 0; }
    else if (i < 4) { j = (wave * 2 + i - 2) * 8 + prow; par = 1; }
    else { j = 128 + prow; par = 0; }
    int row = min(b * T_ + 2 * l0 + 2 * j + par, 2 * T_ - 1);
    xoff[i] = (unsigned)row * 1024u + (unsigned)((pch ^ ((j >> 1) & 7)) * 16);
  }
  unsigned woff[3];
#pragma unroll
  for (int i = 0; i < 3; ++i) {
    int r = (wave * 3 + i) * 8 + prow;
    int tap = r >> 6, k = r & 63;
    woff[i] = (unsigned)(tap * 512 + k) * 1024u + (unsigned)(nt * 128) + (unsigned)((pch ^ (((k >> 1) & 1) << 2)) * 16);
  }

  auto stage_load = [&](int s) {
    char* base = smem + (s & 1) * STAGE_B;
    const char* xs = xb + s * 128;
    const char* wsrc = wb + (size_t)s * 65536;
    if (wave == 0)
      __builtin_amdgcn_global_load_lds((const void*)(xs + xoff[4]), (void*)(base + XE_OFF + 16 * 1024), 16, 0, 0);
#pragma unroll
    for (int i = 0; i < 2; ++i)
      __builtin_amdgcn_global_load_lds((const void*)(xs + xoff[i]), (void*)(base + XE_OFF + (wave * 2 + i) * 1024), 16, 0, 0);
#pragma unroll
    for (int i = 0; i < 3; ++i)
      __builtin_amdgcn_global_load_lds((const void*)(wsrc + woff[i]), (void*)(base + W_OFF + (wave * 3 + i) * 1024), 16, 0, 0);
#pragma unroll
    for (int i = 0; i < 2; ++i)
      __builtin_amdgcn_global_load_lds((const void*)(xs + xoff[2 + i]), (void*)(base + XO_OFF + (wave * 2 + i) * 1024), 16, 0, 0);
  };

  const unsigned sbase = (unsigned)(size_t)(LAS char*)smem;
  const int blk = (lane >> 4) & 1, q = (lane & 15) >> 2, p = lane & 3;
  unsigned aaddr[2][2];
#pragma unroll
  for (int tv = 0; tv < 2; ++tv)
#pragma unroll
    for (int k2 = 0; k2 < 2; ++k2) {
      int j = wm * 64 + (lane & 31) + tv;
      int ch = 2 * (kh * 2 + k2) + h;
      aaddr[tv][k2] = (unsigned)(j * 128 + ((ch ^ ((j >> 1) & 7)) * 16));
    }
  const int r0 = 16 * (kh * 2) + 8 * h + q;
  const unsigned baddr = (unsigned)(W_OFF + r0 * 128 + ((wn * 64 + 32 * blk + 8 * p) ^ (((r0 >> 1) & 1) << 6)));

  f32x16 acc0 = {}, acc1 = {};

  stage_load(0);
  stage_load(1);

#pragma unroll 1
  for (int s = 0; s < NSTAGE; ++s) {
    if (s < NSTAGE - 1) asm volatile("s_waitcnt vmcnt(7)" ::: "memory");
    else asm volatile("s_waitcnt vmcnt(0)" ::: "memory");
    __builtin_amdgcn_s_barrier();
    const unsigned ub = sbase + (s & 1) * STAGE_B;
    bf16x8 a0[6], a1[6];
    bf16x4 t0[6], t1[6];
#pragma unroll
    for (int st = 0; st < 6; ++st) {
      const int tap = st >> 1, k2 = st & 1;
      const unsigned aa = ub + aaddr[tap >> 1][k2];
      const int pl = (tap & 1) ? XO_OFF : XE_OFF;
      a0[st] = rd128(aa, pl);
      a1[st] = rd128(aa, pl + 4096);
      t0[st] = tr_read(ub + baddr, tap * 8192 + k2 * 2048);
      t1[st] = tr_read(ub + baddr, tap * 8192 + k2 * 2048 + 512);
    }
#pragma unroll
    for (int st = 0; st < 6; ++st) {
      if (st == 0) asm volatile("s_waitcnt lgkmcnt(15)" ::: "memory");
      if (st == 1) asm volatile("s_waitcnt lgkmcnt(15)" ::: "memory");
      if (st == 2) asm volatile("s_waitcnt lgkmcnt(12)" ::: "memory");
      if (st == 3) asm volatile("s_waitcnt lgkmcnt(8)" ::: "memory");
      if (st == 4) asm volatile("s_waitcnt lgkmcnt(4)" ::: "memory");
      if (st == 5) asm volatile("s_waitcnt lgkmcnt(0)" ::: "memory");
      __builtin_amdgcn_sched_barrier(0);
      bf16x8 bb = __builtin_shufflevector(t0[st], t1[st], 0, 1, 2, 3, 4, 5, 6, 7);
      acc0 = __builtin_amdgcn_mfma_f32_32x32x16_bf16(a0[st], bb, acc0, 0, 0, 0);
      acc1 = __builtin_amdgcn_mfma_f32_32x32x16_bf16(a1[st], bb, acc1, 0, 0, 0);
      __builtin_amdgcn_sched_barrier(0);
    }
    __builtin_amdgcn_s_barrier();
    if (s + 2 < NSTAGE) stage_load(s + 2);
  }
  STAMPT(1);

  {
    f32x4* xch = (f32x4*)smem;
    f32x16 snd = kh ? acc0 : acc1;
#pragma unroll
    for (int i = 0; i < 4; ++i)
      xch[(wave * 4 + i) * 64 + lane] = (f32x4){snd[4 * i], snd[4 * i + 1], snd[4 * i + 2], snd[4 * i + 3]};
    __syncthreads();
  }
  float v[16];
  {
    const f32x4* xch = (const f32x4*)smem;
    f32x16 keep = kh ? acc1 : acc0;
#pragma unroll
    for (int i = 0; i < 4; ++i) {
      f32x4 o = xch[((wave ^ 4) * 4 + i) * 64 + lane];
#pragma unroll
      for (int e = 0; e < 4; ++e) v[4 * i + e] = keep[4 * i + e] + o[e] + bia;
    }
  }
  const int rbase = wm * 64 + kh * 32 + 4 * h;
  float cnt = 0.f, sum = 0.f;
#pragma unroll
  for (int r = 0; r < 16; ++r) {
    bool ok = rbase + (r & 3) + 8 * (r >> 2) < nvalid;
    cnt += ok ? 1.f : 0.f;
    sum += ok ? v[r] : 0.f;
  }
  float mean = cnt > 0.f ? sum * __builtin_amdgcn_rcpf(cnt) : 0.f;
  float m2 = 0.f;
#pragma unroll
  for (int r = 0; r < 16; ++r) {
    float d = v[r] - mean;
    m2 += rbase + (r & 3) + 8 * (r >> 2) < nvalid ? d * d : 0.f;
  }
  {
    float nb = __shfl_xor(cnt, 32), mb = __shfl_xor(mean, 32), qb = __shfl_xor(m2, 32);
    merge(cnt, mean, m2, nb, mb, qb);
  }
  float* sA = (float*)(smem + 32768);
  float2* sB = (float2*)(smem + 40960);
  char* sO = smem + 49152 + 16384;
  if (h == 0) { float* d = sA + ((wm * 2 + kh) * 64 + col) * 4; d[0] = cnt; d[1] = mean; d[2] = m2; }
  __syncthreads();
  gu64* gran = (gu64*)((char*)ws + GRAN_OFF) + (size_t)grp * 2048;
  if (tid < 128) {
    int c = tid >> 1;
    float n0 = sA[c * 4], me = sA[c * 4 + 1], q0 = sA[c * 4 + 2];
#pragma unroll
    for (int g = 1; g < 4; ++g)
      merge(n0, me, q0, sA[(g * 64 + c) * 4], sA[(g * 64 + c) * 4 + 1], sA[(g * 64 + c) * 4 + 2]);
    float val = (tid & 1) ? q0 : me;
    __hip_atomic_store(gran + (c * 16 + mt) * 2 + (tid & 1),
                       ((unsigned long long)epoch << 32) | __float_as_uint(val), RLX_AGENT);
  }
  STAMPT(2);
  if (wave == 7) {
    gu64* g = gran + (63 * 16 + (lane & 15)) * 2 + 1;
    for (unsigned spins = 0; spins < (1u << 22); ++spins) {
      unsigned long long xx = __hip_atomic_load(g, RLX_AGENT);
      if (__all((unsigned)(xx >> 32) == epoch)) break;
      __builtin_amdgcn_s_sleep(1);
    }
  }
  __syncthreads();
  {
    gu64* g = gran + tid * 4;
    unsigned vv[4];
    for (unsigned spins = 0;; ++spins) {
      bool ok = true;
#pragma unroll
      for (int k = 0; k < 4; ++k) {
        unsigned long long xx = __hip_atomic_load(g + k, RLX_AGENT);
        vv[k] = (unsigned)xx;
        ok &= (unsigned)(xx >> 32) == epoch;
      }
      if (__all(ok) || spins > (1u << 22)) break;
      __builtin_amdgcn_s_sleep(1);
    }
#pragma unroll
    for (int k = 0; k < 2; ++k)
      sB[((tid & 7) * 2 + k) * 64 + (tid >> 3)] = make_float2(__uint_as_float(vv[2 * k]), __uint_as_float(vv[2 * k + 1]));
  }
  __syncthreads();
  STAMPT(3);
  if (tid == 0) __hip_atomic_store(epoch_p, epoch, RLX_AGENT);
  float gmean, rstd;
  {
    float2 st[16];
#pragma unroll
    for (int i = 0; i < 16; ++i) st[i] = sB[i * 64 + col];
    float sm = 0.f;
#pragma unroll
    for (int i = 0; i < 16; ++i) sm += st[i].x * (i == 15 ? (float)(L_ - 15 * 128) : 128.f);
    gmean = sm * (1.0f / L_);
    float M2 = 0.f;
#pragma unroll
    for (int i = 0; i < 16; ++i) {
      float d = st[i].x - gmean;
      M2 += st[i].y + d * d * (i == 15 ? (float)(L_ - 15 * 128) : 128.f);
    }
    rstd = rsqrtf(M2 * (1.0f / L_) + 1e-5f);
  }
  const float sc = rstd * gam;
  const float sh = bet - gmean * sc;
#pragma unroll
  for (int r = 0; r < 16; ++r) {
    int row = rbase + (r & 3) + 8 * (r >> 2);
    float yn = v[r] * sc + sh;
    float z = yn * (-2.302208198f - 0.1029432397f * yn * yn);
    float e = __builtin_amdgcn_exp2f(z);
    float o = yn * __builtin_amdgcn_rcpf(1.f + e);
    *(__hip_bfloat16*)(sO + row * 128 + col * 2) = __float2bfloat16(o);
  }
  __syncthreads();
#pragma unroll
  for (int i = 0; i < 2; ++i) {
    int row = i * 64 + (tid >> 3);
    if (row < nvalid) {
      u32x4 d = *(const u32x4*)(sO + row * 128 + (tid & 7) * 16);
      *(u32x4*)((char*)out + ((size_t)(b * L_ + l0 + row) * 512 + nt * 64) * 2 + (tid & 7) * 16) = d;
    }
  }
  STAMPT(4);
}

extern "C" void kernel_launch(void* const* d_in, const int* in_sizes, int n_in,
                              void* d_out, int out_size, void* d_ws, size_t ws_size,
                              hipStream_t stream) {
  conv_stem_kernel<<<256, 512, 0, stream>>>(
      (const __hip_bfloat16*)d_in[0], (const __hip_bfloat16*)d_in[1], (const __hip_bfloat16*)d_in[2],
      (const __hip_bfloat16*)d_in[3], (const __hip_bfloat16*)d_in[4], (__hip_bfloat16*)d_out, (unsigned*)d_ws);
}
```

```cpp
#include <hip/hip_runtime.h>
#include <hip/hip_bf16.h>

typedef __attribute__((ext_vector_type(4))) __bf16 bf16x4;
typedef __attribute__((ext_vector_type(8))) __bf16 bf16x8;
typedef __attribute__((ext_vector_type(16))) float f32x16;
typedef __attribute__((ext_vector_type(4))) float f32x4;
typedef __attribute__((ext_vector_type(4))) unsigned u32x4;
#define GAS __attribute__((address_space(1)))
#define LAS __attribute__((address_space(3)))
typedef GAS unsigned gu32;
typedef GAS unsigned long long gu64;
#define RLX_AGENT __ATOMIC_RELAXED, __HIP_MEMORY_SCOPE_AGENT

constexpr int T_ = 4000, D_ = 512, L_ = 1999;
constexpr int XE_OFF = 0, XO_OFF = 17408, W_OFF = 33792, STAGE_B = 58368;
constexpr int NSTAGE = 8;
constexpr int GRAN_OFF = 8192;

#define STAMPT(i)

__device__ __forceinline__ bf16x4 tr_read(unsigned addr, int off) {
  bf16x4 r;
  asm volatile("ds_read_b64_tr_b16 %0, %1 offset:%2" : "=v"(r) : "v"(addr), "i"(off));
  return r;
}
__device__ __forceinline__ bf16x8 rd128(unsigned addr, int off) {
  bf16x8 r;
  asm volatile("ds_read_b128 %0, %1 offset:%2" : "=v"(r) : "v"(addr), "i"(off));
  return r;
}

__device__ __forceinline__ void merge(float& n, float& mean, float& m2, float nb, float meanb, float m2b) {
  float nt = n + nb;
  float inv = nt > 0.f ? __builtin_amdgcn_rcpf(nt) : 0.f;
  float d = meanb - mean;
  mean = mean + d * nb * inv;
  m2 = m2 + m2b + d * d * n * nb * inv;
  n = nt;
}

__global__ void __launch_bounds__(512, 2)
conv_stem_kernel(const __hip_bfloat16* __restrict__ x, const __hip_bfloat16* __restrict__ w,
                 const __hip_bfloat16* __restrict__ bias, const __hip_bfloat16* __restrict__ gamma,
                 const __hip_bfloat16* __restrict__ beta, __hip_bfloat16* __restrict__ out,
                 unsigned* __restrict__ ws) {
  __shared__ __attribute__((aligned(16))) char smem[2 * STAGE_B];
  const int tid = threadIdx.x;
  const int lane = tid & 63;
  const int wave = __builtin_amdgcn_readfirstlane(tid >> 6);
  const int wg = blockIdx.x;
  const int xcd = wg & 7, idx = wg >> 3;
  const int b = xcd >> 2, mt = (xcd & 3) * 4 + (idx >> 3), nt = idx & 7;
  const int grp = b * 8 + nt;
  const int l0 = mt * 128;
  const int nvalid = min(128, L_ - l0);
  STAMPT(0);

  gu32* epoch_p = (gu32*)ws + grp * 64;
  const unsigned epoch = __hip_atomic_load(epoch_p, RLX_AGENT) + 1u;

  const int kh = wave >> 2, wm = (wave >> 1) & 1, wn = wave & 1;
  const int col = wn * 32 + (lane & 31);
  const int h = lane >> 5;
  const float bia = __bfloat162float(bias[nt * 64 + col]);
  const float gam = __bfloat162float(gamma[nt * 64 + col]);
  const float bet = __bfloat162float(beta[nt * 64 + col]);

  const int prow = lane >> 3, pch = lane & 7;
  const char* xb = (const char*)x;
  const char* wb = (const char*)w;
  unsigned xoff[5];
#pragma unroll
  for (int i = 0; i < 5; ++i) {
    int j, par;
    if (i < 2) { j = (wave * 2 + i) * 8 + prow; par = 0; }
    else if (i < 4) { j = (wave * 2 + i - 2) * 8 + prow; par = 1; }
    else { j = 128 + prow; par = 0; }
    int row = min(b * T_ + 2 * l0 + 2 * j + par, 2 * T_ - 1);
    xoff[i] = (unsigned)row * 1024u + (unsigned)((pch ^ ((j >> 1) & 7)) * 16);
  }
  unsigned woff[3];
#pragma unroll
  for (int i = 0; i < 3; ++i) {
    int r = (wave * 3 + i) * 8 + prow;
    int tap = r >> 6, k = r & 63;
    woff[i] = (unsigned)(tap * 512 + k) * 1024u + (unsigned)(nt * 128) + (unsigned)((pch ^ (((k >> 1) & 1) << 2)) * 16);
  }

  const int rot = (mt + nt) & 7;
  auto stage_load = [&](int s) {
    char* base = smem + (s & 1) * STAGE_B;
    const int cs = (s + rot) & 7;
    const char* xs = xb + cs * 128;
    const char* wsrc = wb + (size_t)cs * 65536;
    if (wave == 0)
      __builtin_amdgcn_global_load_lds((const void*)(xs + xoff[4]), (void*)(base + XE_OFF + 16 * 1024), 16, 0, 0);
#pragma unroll
    for (int i = 0; i < 2; ++i)
      __builtin_amdgcn_global_load_lds((const void*)(xs + xoff[i]), (void*)(base + XE_OFF + (wave * 2 + i) * 1024), 16, 0, 0);
#pragma unroll
    for (int i = 0; i < 3; ++i)
      __builtin_amdgcn_global_load_lds((const void*)(wsrc + woff[i]), (void*)(base + W_OFF + (wave * 3 + i) * 1024), 16, 0, 0);
#pragma unroll
    for (int i = 0; i < 2; ++i)
      __builtin_amdgcn_global_load_lds((const void*)(xs + xoff[2 + i]), (void*)(base + XO_OFF + (wave * 2 + i) * 1024), 16, 0, 0);
  };

  const unsigned sbase = (unsigned)(size_t)(LAS char*)smem;
  const int blk = (lane >> 4) & 1, q = (lane & 15) >> 2, p = lane & 3;
  unsigned aaddr[2][2];
#pragma unroll
  for (int tv = 0; tv < 2; ++tv)
#pragma unroll
    for (int k2 = 0; k2 < 2; ++k2) {
      int j = wm * 64 + (lane & 31) + tv;
      int ch = 2 * (kh * 2 + k2) + h;
      aaddr[tv][k2] = (unsigned)(j * 128 + ((ch ^ ((j >> 1) & 7)) * 16));
    }
  const int r0 = 16 * (kh * 2) + 8 * h + q;
  const unsigned baddr = (unsigned)(W_OFF + r0 * 128 + ((wn * 64 + 32 * blk + 8 * p) ^ (((r0 >> 1) & 1) << 6)));

  f32x16 acc0 = {}, acc1 = {};

  stage_load(0);
  stage_load(1);

#pragma unroll 1
  for (int s = 0; s < NSTAGE; ++s) {
    if (s < NSTAGE - 1) asm volatile("s_waitcnt vmcnt(7)" ::: "memory");
    else asm volatile("s_waitcnt vmcnt(0)" ::: "memory");
    __builtin_amdgcn_s_barrier();
    const unsigned ub = sbase + (s & 1) * STAGE_B;
    bf16x8 a0[6], a1[6];
    bf16x4 t0[6], t1[6];
#pragma unroll
    for (int st = 0; st < 6; ++st) {
      const int tap = st >> 1, k2 = st & 1;
      const unsigned aa = ub + aaddr[tap >> 1][k2];
      const int pl = (tap & 1) ? XO_OFF : XE_OFF;
      a0[st] = rd128(aa, pl);
      a1[st] = rd128(aa, pl + 4096);
      t0[st] = tr_read(ub + baddr, tap * 8192 + k2 * 2048);
      t1[st] = tr_read(ub + baddr, tap * 8192 + k2 * 2048 + 512);
    }
#pragma unroll
    for (int st = 0; st < 6; ++st) {
      if (st == 0) asm volatile("s_waitcnt lgkmcnt(15)" ::: "memory");
      if (st == 1) asm volatile("s_waitcnt lgkmcnt(15)" ::: "memory");
      if (st == 2) asm volatile("s_waitcnt lgkmcnt(12)" ::: "memory");
      if (st == 3) asm volatile("s_waitcnt lgkmcnt(8)" ::: "memory");
      if (st == 4) asm volatile("s_waitcnt lgkmcnt(4)" ::: "memory");
      if (st == 5) asm volatile("s_waitcnt lgkmcnt(0)" ::: "memory");
      __builtin_amdgcn_sched_barrier(0);
      bf16x8 bb = __builtin_shufflevector(t0[st], t1[st], 0, 1, 2, 3, 4, 5, 6, 7);
      acc0 = __builtin_amdgcn_mfma_f32_32x32x16_bf16(a0[st], bb, acc0, 0, 0, 0);
      acc1 = __builtin_amdgcn_mfma_f32_32x32x16_bf16(a1[st], bb, acc1, 0, 0, 0);
      __builtin_amdgcn_sched_barrier(0);
    }
    __builtin_amdgcn_s_barrier();
    if (s + 2 < NSTAGE) stage_load(s + 2);
  }
  STAMPT(1);

  {
    f32x4* xch = (f32x4*)smem;
    f32x16 snd = kh ? acc0 : acc1;
#pragma unroll
    for (int i = 0; i < 4; ++i)
      xch[(wave * 4 + i) * 64 + lane] = (f32x4){snd[4 * i], snd[4 * i + 1], snd[4 * i + 2], snd[4 * i + 3]};
    __syncthreads();
  }
  float v[16];
  {
    const f32x4* xch = (const f32x4*)smem;
    f32x16 keep = kh ? acc1 : acc0;
#pragma unroll
    for (int i = 0; i < 4; ++i) {
      f32x4 o = xch[((wave ^ 4) * 4 + i) * 64 + lane];
#pragma unroll
      for (int e = 0; e < 4; ++e) v[4 * i + e] = keep[4 * i + e] + o[e] + bia;
    }
  }
  const int rbase = wm * 64 + kh * 32 + 4 * h;
  float cnt = 0.f, sum = 0.f;
#pragma unroll
  for (int r = 0; r < 16; ++r) {
    bool ok = rbase + (r & 3) + 8 * (r >> 2) < nvalid;
    cnt += ok ? 1.f : 0.f;
    sum += ok ? v[r] : 0.f;
  }
  float mean = cnt > 0.f ? sum * __builtin_amdgcn_rcpf(cnt) : 0.f;
  float m2 = 0.f;
#pragma unroll
  for (int r = 0; r < 16; ++r) {
    float d = v[r] - mean;
    m2 += rbase + (r & 3) + 8 * (r >> 2) < nvalid ? d * d : 0.f;
  }
  {
    float nb = __shfl_xor(cnt, 32), mb = __shfl_xor(mean, 32), qb = __shfl_xor(m2, 32);
    merge(cnt, mean, m2, nb, mb, qb);
  }
  float* sA = (float*)(smem + 32768);
  float2* sB = (float2*)(smem + 40960);
  char* sO = smem + 49152 + 16384;
  if (h == 0) { float* d = sA + ((wm * 2 + kh) * 64 + col) * 4; d[0] = cnt; d[1] = mean; d[2] = m2; }
  __syncthreads();
  gu64* gran = (gu64*)((char*)ws + GRAN_OFF) + (size_t)grp * 2048;
  if (tid < 128) {
    int c = tid >> 1;
    float n0 = sA[c * 4], me = sA[c * 4 + 1], q0 = sA[c * 4 + 2];
#pragma unroll
    for (int g = 1; g < 4; ++g)
      merge(n0, me, q0, sA[(g * 64 + c) * 4], sA[(g * 64 + c) * 4 + 1], sA[(g * 64 + c) * 4 + 2]);
    float val = (tid & 1) ? q0 : me;
    __hip_atomic_store(gran + (c * 16 + mt) * 2 + (tid & 1),
                       ((unsigned long long)epoch << 32) | __float_as_uint(val), RLX_AGENT);
  }
  STAMPT(2);
  if (wave == 7) {
    gu64* g = gran + (63 * 16 + (lane & 15)) * 2 + 1;
    for (unsigned spins = 0; spins < (1u << 22); ++spins) {
      unsigned long long xx = __hip_atomic_load(g, RLX_AGENT);
      if (__all((unsigned)(xx >> 32) == epoch)) break;
      __builtin_amdgcn_s_sleep(1);
    }
  }
  __syncthreads();
  {
    gu64* g = gran + tid * 4;
    unsigned vv[4];
    for (unsigned spins = 0;; ++spins) {
      bool ok = true;
#pragma unroll
      for (int k = 0; k < 4; ++k) {
        unsigned long long xx = __hip_atomic_load(g + k, RLX_AGENT);
        vv[k] = (unsigned)xx;
        ok &= (unsigned)(xx >> 32) == epoch;
      }
      if (__all(ok) || spins > (1u << 22)) break;
      __builtin_amdgcn_s_sleep(1);
    }
#pragma unroll
    for (int k = 0; k < 2; ++k)
      sB[((tid & 7) * 2 + k) * 64 + (tid >> 3)] = make_float2(__uint_as_float(vv[2 * k]), __uint_as_float(vv[2 * k + 1]));
  }
  __syncthreads();
  STAMPT(3);
  if (tid == 0) __hip_atomic_store(epoch_p, epoch, RLX_AGENT);
  float gmean, rstd;
  {
    float2 st[16];
#pragma unroll
    for (int i = 0; i < 16; ++i) st[i] = sB[i * 64 + col];
    float sm = 0.f;
#pragma unroll
    for (int i = 0; i < 16; ++i) sm += st[i].x * (i == 15 ? (float)(L_ - 15 * 128) : 128.f);
    gmean = sm * (1.0f / L_);
    float M2 = 0.f;
#pragma unroll
    for (int i = 0; i < 16; ++i) {
      float d = st[i].x - gmean;
      M2 += st[i].y + d * d * (i == 15 ? (float)(L_ - 15 * 128) : 128.f);
    }
    rstd = rsqrtf(M2 * (1.0f / L_) + 1e-5f);
  }
  const float sc = rstd * gam;
  const float sh = bet - gmean * sc;
#pragma unroll
  for (int r = 0; r < 16; ++r) {
    int row = rbase + (r & 3) + 8 * (r >> 2);
    float yn = v[r] * sc + sh;
    float z = yn * (-2.302208198f - 0.1029432397f * yn * yn);
    float e = __builtin_amdgcn_exp2f(z);
    float o = yn * __builtin_amdgcn_rcpf(1.f + e);
    *(__hip_bfloat16*)(sO + row * 128 + col * 2) = __float2bfloat16(o);
  }
  __syncthreads();
#pragma unroll
  for (int i = 0; i < 2; ++i) {
    int row = i * 64 + (tid >> 3);
    if (row < nvalid) {
      u32x4 d = *(const u32x4*)(sO + row * 128 + (tid & 7) * 16);
      *(u32x4*)((char*)out + ((size_t)(b * L_ + l0 + row) * 512 + nt * 64) * 2 + (tid & 7) * 16) = d;
    }
  }
  STAMPT(4);
}

extern "C" void kernel_launch(void* const* d_in, const int* in_sizes, int n_in,
                              void* d_out, int out_size, void* d_ws, size_t ws_size,
                              hipStream_t stream) {
  conv_stem_kernel<<<256, 512, 0, stream>>>(
      (const __hip_bfloat16*)d_in[0], (const __hip_bfloat16*)d_in[1], (const __hip_bfloat16*)d_in[2],
      (const __hip_bfloat16*)d_in[3], (const __hip_bfloat16*)d_in[4], (__hip_bfloat16*)d_out, (unsigned*)d_ws);
}
```
